```python
import torch

def _softmax(x, axis=-1):
    return torch.softmax(x, dim=axis)
def _gelu(x):
    return torch.nn.functional.gelu(x, approximate="tanh")
def _silu(x):
    return torch.nn.functional.silu(x)
def _sigmoid(x):
    return torch.sigmoid(x)
def _layernorm(x, axis=-1, eps=1e-5):
    m = x.mean(dim=axis, keepdim=True)
    v = x.var(dim=axis, keepdim=True, unbiased=False)
    return (x - m) / torch.sqrt(v + eps)
def _scatter_add(tgt, idx, src, axis=0):
    return tgt.clone().index_add_(axis, idx, src)
def _segment_sum(src, seg, n):
    out = torch.zeros((n,) + src.shape[1:], dtype=src.dtype)
    return out.index_add_(0, seg, src)
def _pad(x, pw):
    flat = tuple(v for pair in reversed(pw) for v in pair)
    return torch.nn.functional.pad(x, flat)

kernel_name = 'standalone__low_rank_proj_linformer__sampling'

def setup_inputs():

    _w = (lambda a: (lambda t: t.to(torch.bfloat16) if t.is_floating_point() else t)(torch.as_tensor(a)))
    B, H, Sq, Skv, Dh, R = 16, 32, 32, 4096, 128, 16
    return {
        "q": _w((lambda *s: torch.randn(*s, dtype=torch.bfloat16) * 0.8)(B, H, Sq, Dh)),
        "k": _w((lambda *s: torch.randn(*s, dtype=torch.bfloat16) * 0.8)(B, H, Skv, Dh)),
        "v": _w((lambda *s: torch.randn(*s, dtype=torch.bfloat16) * 0.05)(B, H, Skv, Dh)),
        "e_proj": _w((lambda *s: torch.randn(*s, dtype=torch.bfloat16) * 0.8)(R, Skv) * (Skv ** -0.5)),
        "f_proj": _w((lambda *s: torch.randn(*s, dtype=torch.bfloat16) * 0.05)(R, Skv) * (Skv ** -0.5)),
    }

def reference(q, k, v, e_proj, f_proj):
    B, H, Sq, Dh = q.shape
    kp = torch.einsum("rn,bhnd->bhrd", e_proj, k)
    vp = torch.einsum("rn,bhnd->bhrd", f_proj, v)
    scores = torch.einsum("bhqd,bhrd->bhqr", q, kp) * (Dh ** -0.5)
    p = _softmax(scores, axis=-1)
    o = torch.einsum("bhqr,bhrd->bhqd", p, vp)
    return o
```

```cpp
#include <hip/hip_runtime.h>
#include <hip/hip_bf16.h>
#include <stdint.h>

typedef unsigned u32x4 __attribute__((ext_vector_type(4)));
typedef unsigned u32x2 __attribute__((ext_vector_type(2)));
typedef float f32x4 __attribute__((ext_vector_type(4)));
typedef short s16x8 __attribute__((ext_vector_type(8)));
typedef short s16x4 __attribute__((ext_vector_type(4)));
typedef __bf16 bf16x8_t __attribute__((ext_vector_type(8)));

#define NWK 4
#define DEPTH 4
#define AUX 2
#define MINW 2

constexpr int NBH = 512, SQ = 32, SKV = 4096, DH = 128, RK = 16;

__device__ __forceinline__ unsigned cvt_pk_bf16(float lo, float hi) {
    typedef __bf16 bf2 __attribute__((ext_vector_type(2)));
    bf2 r; r[0] = (__bf16)lo; r[1] = (__bf16)hi;
    return __builtin_bit_cast(unsigned, r);
}
__device__ __forceinline__ float bf16_round(float x) {
    return (float)((__bf16)x);
}

template <int kNWK, int kDEPTH, int kAUX>
__global__ void __launch_bounds__(kNWK * 2 * 64, MINW)
linformer_kernel(const unsigned short* __restrict__ q, const unsigned short* __restrict__ k,
                 const unsigned short* __restrict__ v, const unsigned short* __restrict__ ep,
                 const unsigned short* __restrict__ fp, unsigned short* __restrict__ out) {
    constexpr int NW = 2 * kNWK;
    constexpr int NCH = (SKV / 32) / kNWK;
    static_assert(NCH % kDEPTH == 0, "");
    __shared__ f32x4 red[NW * 8 * 64];

    const int bh = blockIdx.x;
    const int tid = threadIdx.x, lane = tid & 63;
    const int wave = __builtin_amdgcn_readfirstlane(tid >> 6);
    const int isV = wave / kNWK, sub = wave % kNWK;
    const int g = lane >> 4, c = lane & 15;

    const unsigned short* X = (isV ? v : k) + (size_t)bh * (SKV * DH);
    const unsigned short* P = isV ? fp : ep;
    const __amdgpu_buffer_rsrc_t rX = __builtin_amdgcn_make_buffer_rsrc((void*)X, 0, SKV * DH * 2, 0x00020000);
    const __amdgpu_buffer_rsrc_t rP = __builtin_amdgcn_make_buffer_rsrc((void*)P, 0, RK * SKV * 2, 0x00020000);
    const int voffX = g * 2048 + c * 16;
    const int voffP = c * (SKV * 2) + g * 16;

    u32x4 qf[4];
    if (wave < 2) {
        const unsigned short* qp = q + ((size_t)bh * SQ + 16 * wave + c) * DH + 32 * g;
#pragma unroll
        for (int s = 0; s < 4; ++s) qf[s] = *(const u32x4*)(qp + 8 * s);
    }

    u32x4 xb[kDEPTH][8], ab[kDEPTH];
    f32x4 acc[8];
#pragma unroll
    for (int e = 0; e < 8; ++e) acc[e] = f32x4{0.f, 0.f, 0.f, 0.f};

#define ISSUE(s_, ch_) do { const int cc_ = (ch_) * kNWK + sub; \
        ab[s_] = __builtin_amdgcn_raw_buffer_load_b128(rP, voffP, cc_ * 64, 0); \
        _Pragma("unroll") for (int i = 0; i < 8; ++i) xb[s_][i] = __builtin_amdgcn_raw_buffer_load_b128(rX, voffX + i * 256, cc_ * 8192, kAUX); } while (0)
#define CONSUME(s_, SWAP) do { \
        _Pragma("unroll") for (int w = 0; w < 4; ++w) { u32x4 lo, hi; \
            _Pragma("unroll") for (int m = 0; m < 4; ++m) { \
                lo[m] = __builtin_amdgcn_perm(xb[s_][2 * m + 1][w], xb[s_][2 * m][w], 0x05040100u); \
                hi[m] = __builtin_amdgcn_perm(xb[s_][2 * m + 1][w], xb[s_][2 * m][w], 0x07060302u); } \
            if (SWAP) { \
                acc[2 * w]     = __builtin_amdgcn_mfma_f32_16x16x32_bf16(__builtin_bit_cast(bf16x8_t, lo), __builtin_bit_cast(bf16x8_t, ab[s_]), acc[2 * w], 0, 0, 0); \
                acc[2 * w + 1] = __builtin_amdgcn_mfma_f32_16x16x32_bf16(__builtin_bit_cast(bf16x8_t, hi), __builtin_bit_cast(bf16x8_t, ab[s_]), acc[2 * w + 1], 0, 0, 0); \
            } else { \
                acc[2 * w]     = __builtin_amdgcn_mfma_f32_16x16x32_bf16(__builtin_bit_cast(bf16x8_t, ab[s_]), __builtin_bit_cast(bf16x8_t, lo), acc[2 * w], 0, 0, 0); \
                acc[2 * w + 1] = __builtin_amdgcn_mfma_f32_16x16x32_bf16(__builtin_bit_cast(bf16x8_t, ab[s_]), __builtin_bit_cast(bf16x8_t, hi), acc[2 * w + 1], 0, 0, 0); \
            } } } while (0)

#define MAINLOOP(SWAP) do { \
        _Pragma("unroll") for (int s = 0; s < kDEPTH; ++s) { ISSUE(s, s); __builtin_amdgcn_sched_barrier(0); } \
        __builtin_amdgcn_sched_barrier(0); \
        for (int it = 0; it < NCH - kDEPTH; it += kDEPTH) { \
            _Pragma("unroll") for (int s = 0; s < kDEPTH; ++s) { \
                CONSUME(s, SWAP); __builtin_amdgcn_sched_barrier(0); \
                ISSUE(s, it + s + kDEPTH); __builtin_amdgcn_sched_barrier(0); } } \
        _Pragma("unroll") for (int s = 0; s < kDEPTH; ++s) { CONSUME(s, SWAP); } } while (0)

    if (isV) MAINLOOP(false); else MAINLOOP(true);

#pragma unroll
    for (int e = 0; e < 8; ++e) red[(wave * 8 + e) * 64 + lane] = acc[e];
    __syncthreads();
    if (wave < 2) {
        f32x4 kk[8];
#pragma unroll
        for (int e = 0; e < 8; ++e) {
            f32x4 t = red[(0 * 8 + e) * 64 + lane];
#pragma unroll
            for (int w = 1; w < kNWK; ++w) t += red[(w * 8 + e) * 64 + lane];
            kk[e] = t;
        }
        f32x4 st = f32x4{0.f, 0.f, 0.f, 0.f};
#pragma unroll
        for (int s = 0; s < 4; ++s) {
            u32x4 a;
#pragma unroll
            for (int m = 0; m < 4; ++m) a[m] = cvt_pk_bf16(kk[2 * m][s], kk[2 * m + 1][s]);
            st = __builtin_amdgcn_mfma_f32_16x16x32_bf16(__builtin_bit_cast(bf16x8_t, a), __builtin_bit_cast(bf16x8_t, qf[s]), st, 0, 0, 0);
        }
        float t[4];
#pragma unroll
        for (int i = 0; i < 4; ++i) t[i] = bf16_round(bf16_round(st[i]) * 0.08838834764831845f);
        float mx = fmaxf(fmaxf(t[0], t[1]), fmaxf(t[2], t[3]));
        mx = fmaxf(mx, __shfl_xor(mx, 16));
        mx = fmaxf(mx, __shfl_xor(mx, 32));
        float ex[4], sum = 0.f;
#pragma unroll
        for (int i = 0; i < 4; ++i) { ex[i] = __expf(t[i] - mx); sum += ex[i]; }
        sum += __shfl_xor(sum, 16);
        sum += __shfl_xor(sum, 32);
        const float inv = 1.0f / sum;
        u32x2 pa;
        pa[0] = cvt_pk_bf16(ex[0] * inv, ex[1] * inv);
        pa[1] = cvt_pk_bf16(ex[2] * inv, ex[3] * inv);
        f32x4 o[8];
#pragma unroll
        for (int e = 0; e < 8; ++e) {
            f32x4 t4 = red[((kNWK + 0) * 8 + e) * 64 + lane];
#pragma unroll
            for (int w = 1; w < kNWK; ++w) t4 += red[((kNWK + w) * 8 + e) * 64 + lane];
            u32x2 b;
            b[0] = cvt_pk_bf16(t4[0], t4[1]);
            b[1] = cvt_pk_bf16(t4[2], t4[3]);
            o[e] = __builtin_amdgcn_mfma_f32_16x16x16bf16_1k(__builtin_bit_cast(s16x4, pa), __builtin_bit_cast(s16x4, b), f32x4{0.f, 0.f, 0.f, 0.f}, 0, 0, 0);
        }
        unsigned short* op = out + ((size_t)bh * SQ + 16 * wave + 4 * g) * DH + 8 * c;
#pragma unroll
        for (int i = 0; i < 4; ++i) {
            u32x4 w4;
#pragma unroll
            for (int m = 0; m < 4; ++m) w4[m] = cvt_pk_bf16(o[2 * m][i], o[2 * m + 1][i]);
            *(u32x4*)(op + i * DH) = w4;
        }
    }
}

extern "C" void kernel_launch(void* const* d_in, const int* in_sizes, int n_in,
                              void* d_out, int out_size, void* d_ws, size_t ws_size,
                              hipStream_t stream) {
    const unsigned short* q = (const unsigned short*)d_in[0];
    const unsigned short* k = (const unsigned short*)d_in[1];
    const unsigned short* v = (const unsigned short*)d_in[2];
    const unsigned short* e = (const unsigned short*)d_in[3];
    const unsigned short* f = (const unsigned short*)d_in[4];
    linformer_kernel<NWK, DEPTH, AUX><<<NBH, NWK * 2 * 64, 0, stream>>>(q, k, v, e, f, (unsigned short*)d_out);
}
```

```cpp
#include <hip/hip_runtime.h>
#include <hip/hip_bf16.h>
#include <stdint.h>

typedef unsigned u32x4 __attribute__((ext_vector_type(4)));
typedef unsigned u32x2 __attribute__((ext_vector_type(2)));
typedef float f32x4 __attribute__((ext_vector_type(4)));
typedef short s16x4 __attribute__((ext_vector_type(4)));
typedef __bf16 bf16x8_t __attribute__((ext_vector_type(8)));

#define DEPTH 2
#define AUX 2

constexpr int NBH = 512, SQ = 32, SKV = 4096, DH = 128, RK = 16;
constexpr int NW = 4;
constexpr int NWG = 256;
constexpr int HPW = NBH / NWG;
constexpr int CPS = (SKV / 32) / NW;
constexpr int KROW = 528;
constexpr int KPART = 16 * KROW;
constexpr int VPART = 8 * 64 * 16;
constexpr int HEADBUF = NW * (KPART + VPART);
constexpr int LDS_BYTES = HPW * HEADBUF;

__device__ __forceinline__ unsigned cvt_pk_bf16(float lo, float hi) {
    typedef __bf16 bf2 __attribute__((ext_vector_type(2)));
    bf2 r; r[0] = (__bf16)lo; r[1] = (__bf16)hi;
    return __builtin_bit_cast(unsigned, r);
}
__device__ __forceinline__ float bf16_round(float x) { return (float)((__bf16)x); }

template <int kDEPTH, int kAUX>
__global__ void __launch_bounds__(NW * 64, 1)
linformer_kernel(const unsigned short* __restrict__ q, const unsigned short* __restrict__ k,
                 const unsigned short* __restrict__ v, const unsigned short* __restrict__ ep,
                 const unsigned short* __restrict__ fp, unsigned short* __restrict__ out) {
    static_assert(CPS % kDEPTH == 0, "");
    extern __shared__ __attribute__((aligned(16))) unsigned char lds[];

    const int wg = blockIdx.x;
    const int tid = threadIdx.x, lane = tid & 63;
    const int wave = __builtin_amdgcn_readfirstlane(tid >> 6);
    const int g = lane >> 4, c = lane & 15;

    const int voffX = g * 2048 + c * 16;
    const int voffP = c * (SKV * 2) + g * 16;

    u32x4 xb[kDEPTH][8], ab[kDEPTH];
    f32x4 acc[8];
    u32x4 qf[4];

#define ISSUE(s_, cn_) do { const int n_ = (cn_); const int seg_ = n_ / CPS, cc_ = (n_ % CPS) * NW + wave; \
        const bool isv_ = seg_ & 1; const int live_ = seg_ < 2 * HPW; \
        const __amdgpu_buffer_rsrc_t rX_ = __builtin_amdgcn_make_buffer_rsrc((void*)(isv_ ? v : k), 0, live_ ? NBH * SKV * DH * 2 : 0, 0x00020000); \
        const __amdgpu_buffer_rsrc_t rP_ = __builtin_amdgcn_make_buffer_rsrc((void*)(isv_ ? fp : ep), 0, live_ ? RK * SKV * 2 : 0, 0x00020000); \
        const int sx_ = (wg * HPW + (seg_ >> 1)) * (SKV * DH * 2) + cc_ * 8192; \
        ab[s_] = __builtin_amdgcn_raw_buffer_load_b128(rP_, voffP, cc_ * 64, 0); \
        _Pragma("unroll") for (int i = 0; i < 8; ++i) xb[s_][i] = __builtin_amdgcn_raw_buffer_load_b128(rX_, voffX + i * 256, sx_, kAUX); } while (0)
#define CONSUME(s_) do { \
        _Pragma("unroll") for (int w = 0; w < 4; ++w) { u32x4 lo, hi; \
            _Pragma("unroll") for (int m = 0; m < 4; ++m) { \
                lo[m] = __builtin_amdgcn_perm(xb[s_][2 * m + 1][w], xb[s_][2 * m][w], 0x05040100u); \
                hi[m] = __builtin_amdgcn_perm(xb[s_][2 * m + 1][w], xb[s_][2 * m][w], 0x07060302u); } \
            acc[2 * w]     = __builtin_amdgcn_mfma_f32_16x16x32_bf16(__builtin_bit_cast(bf16x8_t, ab[s_]), __builtin_bit_cast(bf16x8_t, lo), acc[2 * w], 0, 0, 0); \
            acc[2 * w + 1] = __builtin_amdgcn_mfma_f32_16x16x32_bf16(__builtin_bit_cast(bf16x8_t, ab[s_]), __builtin_bit_cast(bf16x8_t, hi), acc[2 * w + 1], 0, 0, 0); \
        } } while (0)

#pragma unroll
    for (int s = 0; s < kDEPTH; ++s) { ISSUE(s, s); __builtin_amdgcn_sched_barrier(0); }

    for (int seg = 0; seg < 2 * HPW; ++seg) {
        const int head = seg >> 1;
        const int bh = wg * HPW + head;
        if (!(seg & 1) && wave < 2) {
            const unsigned short* qp = q + ((size_t)bh * SQ + 16 * wave + c) * DH + 32 * g;
#pragma unroll
            for (int s = 0; s < 4; ++s) qf[s] = *(const u32x4*)(qp + 8 * s);
        }
#pragma unroll
        for (int e = 0; e < 8; ++e) acc[e] = f32x4{0.f, 0.f, 0.f, 0.f};
        for (int it = 0; it < CPS; it += kDEPTH) {
#pragma unroll
            for (int s = 0; s < kDEPTH; ++s) {
                CONSUME(s); __builtin_amdgcn_sched_barrier(0);
                ISSUE(s, seg * CPS + it + s + kDEPTH); __builtin_amdgcn_sched_barrier(0);
            }
        }
        unsigned char* hb = lds + head * HEADBUF;
        if (!(seg & 1)) {
            unsigned char* p = hb + wave * KPART + (4 * g) * KROW + c * 16;
#pragma unroll
            for (int i = 0; i < 4; ++i) {
                *(f32x4*)(p + i * KROW)       = f32x4{acc[0][i], acc[1][i], acc[2][i], acc[3][i]};
                *(f32x4*)(p + i * KROW + 256) = f32x4{acc[4][i], acc[5][i], acc[6][i], acc[7][i]};
            }
        } else {
            unsigned char* p = hb + NW * KPART + wave * VPART + lane * 16;
#pragma unroll
            for (int e = 0; e < 8; ++e) *(f32x4*)(p + e * 1024) = acc[e];
            asm volatile("s_waitcnt lgkmcnt(0)" ::: "memory");
            __builtin_amdgcn_s_barrier();
            asm volatile("" ::: "memory");
            if (wave < 2) {
                f32x4 st = f32x4{0.f, 0.f, 0.f, 0.f};
#pragma unroll
                for (int s = 0; s < 4; ++s) {
                    const unsigned char* p0 = hb + c * KROW + (4 * g + s) * 16;
                    f32x4 a0 = *(const f32x4*)(p0), a1 = *(const f32x4*)(p0 + 256);
#pragma unroll
                    for (int w = 1; w < NW; ++w) { a0 += *(const f32x4*)(p0 + w * KPART); a1 += *(const f32x4*)(p0 + w * KPART + 256); }
                    u32x4 a;
                    a[0] = cvt_pk_bf16(a0[0], a0[1]); a[1] = cvt_pk_bf16(a0[2], a0[3]);
                    a[2] = cvt_pk_bf16(a1[0], a1[1]); a[3] = cvt_pk_bf16(a1[2], a1[3]);
                    st = __builtin_amdgcn_mfma_f32_16x16x32_bf16(__builtin_bit_cast(bf16x8_t, a), __builtin_bit_cast(bf16x8_t, qf[s]), st, 0, 0, 0);
                }
                float t[4];
#pragma unroll
                for (int i = 0; i < 4; ++i) t[i] = bf16_round(bf16_round(st[i]) * 0.08838834764831845f);
                float mx = fmaxf(fmaxf(t[0], t[1]), fmaxf(t[2], t[3]));
                mx = fmaxf(mx, __shfl_xor(mx, 16));
                mx = fmaxf(mx, __shfl_xor(mx, 32));
                float ex[4], sum = 0.f;
#pragma unroll
                for (int i = 0; i < 4; ++i) { ex[i] = __expf(t[i] - mx); sum += ex[i]; }
                sum += __shfl_xor(sum, 16);
                sum += __shfl_xor(sum, 32);
                const float inv = 1.0f / sum;
                u32x2 pa;
                pa[0] = cvt_pk_bf16(ex[0] * inv, ex[1] * inv);
                pa[1] = cvt_pk_bf16(ex[2] * inv, ex[3] * inv);
                f32x4 o[8];
                const unsigned char* pv = hb + NW * KPART + lane * 16;
#pragma unroll
                for (int e = 0; e < 8; ++e) {
                    f32x4 t4 = *(const f32x4*)(pv + e * 1024);
#pragma unroll
                    for (int w = 1; w < NW; ++w) t4 += *(const f32x4*)(pv + w * VPART + e * 1024);
                    u32x2 b;
                    b[0] = cvt_pk_bf16(t4[0], t4[1]);
                    b[1] = cvt_pk_bf16(t4[2], t4[3]);
                    o[e] = __builtin_amdgcn_mfma_f32_16x16x16bf16_1k(__builtin_bit_cast(s16x4, pa), __builtin_bit_cast(s16x4, b), f32x4{0.f, 0.f, 0.f, 0.f}, 0, 0, 0);
                }
                unsigned short* op = out + ((size_t)bh * SQ + 16 * wave + 4 * g) * DH + 8 * c;
#pragma unroll
                for (int i = 0; i < 4; ++i) {
                    u32x4 w4;
#pragma unroll
                    for (int m = 0; m < 4; ++m) w4[m] = cvt_pk_bf16(o[2 * m][i], o[2 * m + 1][i]);
                    *(u32x4*)(op + i * DH) = w4;
                }
            }
        }
    }
}

extern "C" void kernel_launch(void* const* d_in, const int* in_sizes, int n_in,
                              void* d_out, int out_size, void* d_ws, size_t ws_size,
                              hipStream_t stream) {
    const unsigned short* q = (const unsigned short*)d_in[0];
    const unsigned short* k = (const unsigned short*)d_in[1];
    const unsigned short* v = (const unsigned short*)d_in[2];
    const unsigned short* e = (const unsigned short*)d_in[3];
    const unsigned short* f = (const unsigned short*)d_in[4];
    auto kern = linformer_kernel<DEPTH, AUX>;
    hipFuncSetAttribute((const void*)kern, hipFuncAttributeMaxDynamicSharedMemorySize, LDS_BYTES);
    kern<<<NWG, NW * 64, LDS_BYTES, stream>>>(q, k, v, e, f, (unsigned short*)d_out);
}
```

```cpp
#include <hip/hip_runtime.h>
#include <hip/hip_bf16.h>
#include <stdint.h>

typedef unsigned u32x4 __attribute__((ext_vector_type(4)));
typedef unsigned u32x2 __attribute__((ext_vector_type(2)));
typedef float f32x4 __attribute__((ext_vector_type(4)));
typedef short s16x4 __attribute__((ext_vector_type(4)));
typedef __bf16 bf16x8_t __attribute__((ext_vector_type(8)));

#define DEPTH 2
#define AUX 2
#define ABL_NOA 0
#define ABL_NOC 0
#define ABL_LIN 0
#define ABL_NOEPI 0
#define AUXP 0

constexpr int NBH = 512, SQ = 32, SKV = 4096, DH = 128, RK = 16;
constexpr int NW = 4;
constexpr int NWG = 256;
constexpr int HPW = NBH / NWG;
constexpr int CPS = (SKV / 32) / NW;
constexpr int KROW = 528;
constexpr int KPART = 16 * KROW;
constexpr int VPART = 8 * 64 * 16;
constexpr int HEADBUF = NW * (KPART + VPART);
constexpr int LDS_BYTES = HPW * HEADBUF;

__device__ __forceinline__ unsigned cvt_pk_bf16(float lo, float hi) {
    typedef __bf16 bf2 __attribute__((ext_vector_type(2)));
    bf2 r; r[0] = (__bf16)lo; r[1] = (__bf16)hi;
    return __builtin_bit_cast(unsigned, r);
}
__device__ __forceinline__ float bf16_round(float x) { return (float)((__bf16)x); }

template <int kDEPTH, int kAUX>
__global__ void __launch_bounds__(NW * 64, 1)
linformer_kernel(const unsigned short* __restrict__ q, const unsigned short* __restrict__ k,
                 const unsigned short* __restrict__ v, const unsigned short* __restrict__ ep,
                 const unsigned short* __restrict__ fp, unsigned short* __restrict__ out) {
    static_assert(CPS % kDEPTH == 0, "");
    static_assert(HPW * 2 == NW, "epilogue: one (head, q tile) per wave");
    extern __shared__ __attribute__((aligned(16))) unsigned char lds[];

    const int wg = blockIdx.x;
    const int tid = threadIdx.x, lane = tid & 63;
    const int wave = __builtin_amdgcn_readfirstlane(tid >> 6);
    const int g = lane >> 4, c = lane & 15;

    const int voffX = ABL_LIN ? lane * 16 : g * 2048 + c * 16;
    const int voffP = c * (SKV * 2) + g * 16;

    u32x4 xb[kDEPTH][8], ab[kDEPTH];
    f32x4 acc[8];
    u32x4 qf[4];

#define ISSUE(s_, cn_) do { const int n_ = (cn_); const int seg_ = n_ / CPS, cc_ = (n_ % CPS) * NW + wave; \
        const bool isv_ = seg_ & 1; const int live_ = seg_ < 2 * HPW; \
        const __amdgpu_buffer_rsrc_t rX_ = __builtin_amdgcn_make_buffer_rsrc((void*)(isv_ ? v : k), 0, live_ ? NBH * SKV * DH * 2 : 0, 0x00020000); \
        const __amdgpu_buffer_rsrc_t rP_ = __builtin_amdgcn_make_buffer_rsrc((void*)(isv_ ? fp : ep), 0, live_ ? RK * SKV * 2 : 0, 0x00020000); \
        const int sx_ = (wg * HPW + (seg_ >> 1)) * (SKV * DH * 2) + cc_ * 8192; \
        if (!ABL_NOA || n_ < kDEPTH) ab[s_] = __builtin_amdgcn_raw_buffer_load_b128(rP_, voffP, cc_ * 64, AUXP); \
        _Pragma("unroll") for (int i = 0; i < 8; ++i) xb[s_][i] = __builtin_amdgcn_raw_buffer_load_b128(rX_, voffX + (ABL_LIN ? (i & 3) * 1024 : i * 256), sx_ + (ABL_LIN ? (i >> 2) * 4096 : 0), kAUX); } while (0)
#define CONSUME(s_) do { if (ABL_NOC) { _Pragma("unroll") for (int i = 0; i < 8; ++i) asm volatile("" :: "v"(xb[s_][i])); asm volatile("" :: "v"(ab[s_])); } else \
        _Pragma("unroll") for (int w = 0; w < 4; ++w) { u32x4 lo, hi; \
            _Pragma("unroll") for (int m = 0; m < 4; ++m) { \
                lo[m] = __builtin_amdgcn_perm(xb[s_][2 * m + 1][w], xb[s_][2 * m][w], 0x05040100u); \
                hi[m] = __builtin_amdgcn_perm(xb[s_][2 * m + 1][w], xb[s_][2 * m][w], 0x07060302u); } \
            acc[2 * w]     = __builtin_amdgcn_mfma_f32_16x16x32_bf16(__builtin_bit_cast(bf16x8_t, ab[s_]), __builtin_bit_cast(bf16x8_t, lo), acc[2 * w], 0, 0, 0); \
            acc[2 * w + 1] = __builtin_amdgcn_mfma_f32_16x16x32_bf16(__builtin_bit_cast(bf16x8_t, ab[s_]), __builtin_bit_cast(bf16x8_t, hi), acc[2 * w + 1], 0, 0, 0); \
        } } while (0)

#pragma unroll
    for (int s = 0; s < kDEPTH; ++s) { ISSUE(s, s); __builtin_amdgcn_sched_barrier(0); }
    {
        const unsigned short* qp = q + ((size_t)(wg * HPW + (wave >> 1)) * SQ + 16 * (wave & 1) + c) * DH + 32 * g;
#pragma unroll
        for (int s = 0; s < 4; ++s) qf[s] = *(const u32x4*)(qp + 8 * s);
    }
    __builtin_amdgcn_sched_barrier(0);

    for (int seg = 0; seg < 2 * HPW; ++seg) {
#pragma unroll
        for (int e = 0; e < 8; ++e) acc[e] = f32x4{0.f, 0.f, 0.f, 0.f};
        for (int it = 0; it < CPS; it += kDEPTH) {
#pragma unroll
            for (int s = 0; s < kDEPTH; ++s) {
                CONSUME(s); __builtin_amdgcn_sched_barrier(0);
                ISSUE(s, seg * CPS + it + s + kDEPTH); __builtin_amdgcn_sched_barrier(0);
            }
        }
        unsigned char* hb = lds + (seg >> 1) * HEADBUF;
        if (!(seg & 1)) {
            unsigned char* p = hb + wave * KPART + (4 * g) * KROW + c * 16;
#pragma unroll
            for (int i = 0; i < 4; ++i) {
                *(f32x4*)(p + i * KROW)       = f32x4{acc[0][i], acc[1][i], acc[2][i], acc[3][i]};
                *(f32x4*)(p + i * KROW + 256) = f32x4{acc[4][i], acc[5][i], acc[6][i], acc[7][i]};
            }
        } else {
            unsigned char* p = hb + NW * KPART + wave * VPART + lane * 16;
#pragma unroll
            for (int e = 0; e < 8; ++e) *(f32x4*)(p + e * 1024) = acc[e];
        }
    }
    asm volatile("s_waitcnt lgkmcnt(0)" ::: "memory");
    __builtin_amdgcn_s_barrier();
    asm volatile("" ::: "memory");
    if (!ABL_NOEPI) {
        const unsigned char* hb = lds + (wave >> 1) * HEADBUF;
        f32x4 st = f32x4{0.f, 0.f, 0.f, 0.f};
#pragma unroll
        for (int s = 0; s < 4; ++s) {
            const unsigned char* p0 = hb + c * KROW + (4 * g + s) * 16;
            f32x4 a0 = *(const f32x4*)(p0), a1 = *(const f32x4*)(p0 + 256);
#pragma unroll
            for (int w = 1; w < NW; ++w) { a0 += *(const f32x4*)(p0 + w * KPART); a1 += *(const f32x4*)(p0 + w * KPART + 256); }
            u32x4 a;
            a[0] = cvt_pk_bf16(a0[0], a0[1]); a[1] = cvt_pk_bf16(a0[2], a0[3]);
            a[2] = cvt_pk_bf16(a1[0], a1[1]); a[3] = cvt_pk_bf16(a1[2], a1[3]);
            st = __builtin_amdgcn_mfma_f32_16x16x32_bf16(__builtin_bit_cast(bf16x8_t, a), __builtin_bit_cast(bf16x8_t, qf[s]), st, 0, 0, 0);
        }
        float t[4];
#pragma unroll
        for (int i = 0; i < 4; ++i) t[i] = bf16_round(bf16_round(st[i]) * 0.08838834764831845f);
        float mx = fmaxf(fmaxf(t[0], t[1]), fmaxf(t[2], t[3]));
        mx = fmaxf(mx, __shfl_xor(mx, 16));
        mx = fmaxf(mx, __shfl_xor(mx, 32));
        float ex[4], sum = 0.f;
#pragma unroll
        for (int i = 0; i < 4; ++i) { ex[i] = __expf(t[i] - mx); sum += ex[i]; }
        sum += __shfl_xor(sum, 16);
        sum += __shfl_xor(sum, 32);
        const float inv = 1.0f / sum;
        u32x2 pa;
        pa[0] = cvt_pk_bf16(ex[0] * inv, ex[1] * inv);
        pa[1] = cvt_pk_bf16(ex[2] * inv, ex[3] * inv);
        f32x4 o[8];
        const unsigned char* pv = hb + NW * KPART + lane * 16;
#pragma unroll
        for (int e = 0; e < 8; ++e) {
            f32x4 t4 = *(const f32x4*)(pv + e * 1024);
#pragma unroll
            for (int w = 1; w < NW; ++w) t4 += *(const f32x4*)(pv + w * VPART + e * 1024);
            u32x2 b;
            b[0] = cvt_pk_bf16(t4[0], t4[1]);
            b[1] = cvt_pk_bf16(t4[2], t4[3]);
            o[e] = __builtin_amdgcn_mfma_f32_16x16x16bf16_1k(__builtin_bit_cast(s16x4, pa), __builtin_bit_cast(s16x4, b), f32x4{0.f, 0.f, 0.f, 0.f}, 0, 0, 0);
        }
        unsigned short* op = out + ((size_t)(wg * HPW + (wave >> 1)) * SQ + 16 * (wave & 1) + 4 * g) * DH + 8 * c;
#pragma unroll
        for (int i = 0; i < 4; ++i) {
            u32x4 w4;
#pragma unroll
            for (int m = 0; m < 4; ++m) w4[m] = cvt_pk_bf16(o[2 * m][i], o[2 * m + 1][i]);
            *(u32x4*)(op + i * DH) = w4;
        }
    }
}

extern "C" void kernel_launch(void* const* d_in, const int* in_sizes, int n_in,
                              void* d_out, int out_size, void* d_ws, size_t ws_size,
                              hipStream_t stream) {
    const unsigned short* q = (const unsigned short*)d_in[0];
    const unsigned short* k = (const unsigned short*)d_in[1];
    const unsigned short* v = (const unsigned short*)d_in[2];
    const unsigned short* e = (const unsigned short*)d_in[3];
    const unsigned short* f = (const unsigned short*)d_in[4];
    auto kern = linformer_kernel<DEPTH, AUX>;
    (void)hipFuncSetAttribute((const void*)kern, hipFuncAttributeMaxDynamicSharedMemorySize, LDS_BYTES);
    kern<<<NWG, NW * 64, LDS_BYTES, stream>>>(q, k, v, e, f, (unsigned short*)d_out);
}
```

```cpp
#include <hip/hip_runtime.h>
#include <hip/hip_bf16.h>
#include <stdint.h>

typedef unsigned u32x4 __attribute__((ext_vector_type(4)));
typedef unsigned u32x2 __attribute__((ext_vector_type(2)));
typedef float f32x4 __attribute__((ext_vector_type(4)));
typedef short s16x4 __attribute__((ext_vector_type(4)));
typedef __bf16 bf16x8_t __attribute__((ext_vector_type(8)));

#define DEPTH 4
#define AUX 2
#define STAMP 0
#define TAU 1760

constexpr int NBH = 512, SQ = 32, SKV = 4096, DH = 128, RK = 16;
constexpr int NW = 4;
constexpr int NWG = 256;
constexpr int HPW = NBH / NWG;
constexpr int CPS = (SKV / 32) / NW;
constexpr int NC = 2 * HPW * CPS;
constexpr int KROW = 528;
constexpr int KPART = 16 * KROW;
constexpr int VPART = 8 * 64 * 16;
constexpr int HEADBUF = NW * (KPART + VPART);
constexpr int LDS_BYTES = HPW * HEADBUF;

__device__ __forceinline__ unsigned cvt_pk_bf16(float lo, float hi) {
    typedef __bf16 bf2 __attribute__((ext_vector_type(2)));
    bf2 r; r[0] = (__bf16)lo; r[1] = (__bf16)hi;
    return __builtin_bit_cast(unsigned, r);
}
__device__ __forceinline__ float bf16_round(float x) { return (float)((__bf16)x); }

template <int kDEPTH, int kAUX>
__global__ void __launch_bounds__(NW * 64, 1)
linformer_kernel(const unsigned short* __restrict__ q, const unsigned short* __restrict__ k,
                 const unsigned short* __restrict__ v, const unsigned short* __restrict__ ep,
                 const unsigned short* __restrict__ fp, unsigned short* __restrict__ out
                 ) {
    static_assert(CPS % kDEPTH == 0, "");
    static_assert(HPW * 2 == NW, "epilogue: one (head, q tile) per wave");
    extern __shared__ __attribute__((aligned(16))) unsigned char lds[];

    const int wg = blockIdx.x;
    const int tid = threadIdx.x, lane = tid & 63;
    const int wave = __builtin_amdgcn_readfirstlane(tid >> 6);
    const int g = lane >> 4, c = lane & 15;
#define LATE(now_) do {} while (0)

    const int voffX = g * 2048 + c * 16;
    const int voffP = c * (SKV * 2) + g * 16;

    const unsigned t0lo = (unsigned)__builtin_amdgcn_s_memrealtime();
    unsigned nxt = (((unsigned)(wg * NW + wave) * 2654435761u) >> 16) % (unsigned)(TAU ? TAU : 1);
#define PACE() do { if (TAU) { unsigned now_ = ((unsigned)__builtin_amdgcn_s_memrealtime() - t0lo) << 4; \
        while ((int)(now_ - nxt) < 0) { __builtin_amdgcn_s_sleep(1); now_ = ((unsigned)__builtin_amdgcn_s_memrealtime() - t0lo) << 4; } \
        LATE(now_); nxt += TAU; } } while (0)

    u32x4 xb[kDEPTH][8], ab[kDEPTH];
    f32x4 acc[8];

#define ISSUE(s_, cn_) do { const int n_ = (cn_); const int seg_ = n_ / CPS, cc_ = (n_ % CPS) * NW + wave; \
        const bool isv_ = seg_ & 1; \
        const __amdgpu_buffer_rsrc_t rX_ = __builtin_amdgcn_make_buffer_rsrc((void*)(isv_ ? v : k), 0, NBH * SKV * DH * 2, 0x00020000); \
        const __amdgpu_buffer_rsrc_t rP_ = __builtin_amdgcn_make_buffer_rsrc((void*)(isv_ ? fp : ep), 0, RK * SKV * 2, 0x00020000); \
        const int sx_ = (wg * HPW + (seg_ >> 1)) * (SKV * DH * 2) + cc_ * 8192; \
        ab[s_] = __builtin_amdgcn_raw_buffer_load_b128(rP_, voffP, cc_ * 64, 0); \
        _Pragma("unroll") for (int i = 0; i < 8; ++i) xb[s_][i] = __builtin_amdgcn_raw_buffer_load_b128(rX_, voffX + i * 256, sx_, kAUX); } while (0)
#define CONSUME(s_) do { \
        _Pragma("unroll") for (int w = 0; w < 4; ++w) { u32x4 lo, hi; \
            _Pragma("unroll") for (int m = 0; m < 4; ++m) { \
                lo[m] = __builtin_amdgcn_perm(xb[s_][2 * m + 1][w], xb[s_][2 * m][w], 0x05040100u); \
                hi[m] = __builtin_amdgcn_perm(xb[s_][2 * m + 1][w], xb[s_][2 * m][w], 0x07060302u); } \
            acc[2 * w]     = __builtin_amdgcn_mfma_f32_16x16x32_bf16(__builtin_bit_cast(bf16x8_t, ab[s_]), __builtin_bit_cast(bf16x8_t, lo), acc[2 * w], 0, 0, 0); \
            acc[2 * w + 1] = __builtin_amdgcn_mfma_f32_16x16x32_bf16(__builtin_bit_cast(bf16x8_t, ab[s_]), __builtin_bit_cast(bf16x8_t, hi), acc[2 * w + 1], 0, 0, 0); \
        } } while (0)
#define FLUSH_K(head_) do { unsigned char* p = lds + (head_) * HEADBUF + wave * KPART + (4 * g) * KROW + c * 16; \
        _Pragma("unroll") for (int i = 0; i < 4; ++i) { \
            *(f32x4*)(p + i * KROW)       = f32x4{acc[0][i], acc[1][i], acc[2][i], acc[3][i]}; \
            *(f32x4*)(p + i * KROW + 256) = f32x4{acc[4][i], acc[5][i], acc[6][i], acc[7][i]}; } } while (0)
#define FLUSH_V(head_) do { unsigned char* p = lds + (head_) * HEADBUF + NW * KPART + wave * VPART + lane * 16; \
        _Pragma("unroll") for (int e = 0; e < 8; ++e) *(f32x4*)(p + e * 1024) = acc[e]; } while (0)

#pragma unroll
    for (int s = 0; s < kDEPTH; ++s) { PACE(); ISSUE(s, s); __builtin_amdgcn_sched_barrier(0); }
#pragma unroll
    for (int e = 0; e < 8; ++e) acc[e] = f32x4{0.f, 0.f, 0.f, 0.f};

    for (int n = 0; n < NC - kDEPTH; n += kDEPTH) {
#pragma unroll
        for (int s = 0; s < kDEPTH; ++s) {
            CONSUME(s); __builtin_amdgcn_sched_barrier(0);
            PACE();
            ISSUE(s, n + s + kDEPTH); __builtin_amdgcn_sched_barrier(0);
        }
        if (((n + kDEPTH) % CPS) == 0) {
            const int seg = n / CPS;
            if (seg & 1) FLUSH_V(seg >> 1); else FLUSH_K(seg >> 1);
#pragma unroll
            for (int e = 0; e < 8; ++e) acc[e] = f32x4{0.f, 0.f, 0.f, 0.f};
        }
    }
    u32x4 qf[4];
    {
        const unsigned short* qp = q + ((size_t)(wg * HPW + (wave >> 1)) * SQ + 16 * (wave & 1) + c) * DH + 32 * g;
#pragma unroll
        for (int s = 0; s < 4; ++s) qf[s] = *(const u32x4*)(qp + 8 * s);
    }
    __builtin_amdgcn_sched_barrier(0);
#pragma unroll
    for (int s = 0; s < kDEPTH; ++s) { CONSUME(s); }
    FLUSH_V(HPW - 1);
    asm volatile("s_waitcnt lgkmcnt(0)" ::: "memory");
    __builtin_amdgcn_s_barrier();
    asm volatile("" ::: "memory");
    {
        const unsigned char* hb = lds + (wave >> 1) * HEADBUF;
        f32x4 st = f32x4{0.f, 0.f, 0.f, 0.f};
#pragma unroll
        for (int s = 0; s < 4; ++s) {
            const unsigned char* p0 = hb + c * KROW + (4 * g + s) * 16;
            f32x4 a0 = *(const f32x4*)(p0), a1 = *(const f32x4*)(p0 + 256);
#pragma unroll
            for (int w = 1; w < NW; ++w) { a0 += *(const f32x4*)(p0 + w * KPART); a1 += *(const f32x4*)(p0 + w * KPART + 256); }
            u32x4 a;
            a[0] = cvt_pk_bf16(a0[0], a0[1]); a[1] = cvt_pk_bf16(a0[2], a0[3]);
            a[2] = cvt_pk_bf16(a1[0], a1[1]); a[3] = cvt_pk_bf16(a1[2], a1[3]);
            st = __builtin_amdgcn_mfma_f32_16x16x32_bf16(__builtin_bit_cast(bf16x8_t, a), __builtin_bit_cast(bf16x8_t, qf[s]), st, 0, 0, 0);
        }
        float t[4];
#pragma unroll
        for (int i = 0; i < 4; ++i) t[i] = bf16_round(bf16_round(st[i]) * 0.08838834764831845f);
        float mx = fmaxf(fmaxf(t[0], t[1]), fmaxf(t[2], t[3]));
        mx = fmaxf(mx, __shfl_xor(mx, 16));
        mx = fmaxf(mx, __shfl_xor(mx, 32));
        float ex[4], sum = 0.f;
#pragma unroll
        for (int i = 0; i < 4; ++i) { ex[i] = __expf(t[i] - mx); sum += ex[i]; }
        sum += __shfl_xor(sum, 16);
        sum += __shfl_xor(sum, 32);
        const float inv = 1.0f / sum;
        u32x2 pa;
        pa[0] = cvt_pk_bf16(ex[0] * inv, ex[1] * inv);
        pa[1] = cvt_pk_bf16(ex[2] * inv, ex[3] * inv);
        f32x4 o[8];
        const unsigned char* pv = hb + NW * KPART + lane * 16;
#pragma unroll
        for (int e = 0; e < 8; ++e) {
            f32x4 t4 = *(const f32x4*)(pv + e * 1024);
#pragma unroll
            for (int w = 1; w < NW; ++w) t4 += *(const f32x4*)(pv + w * VPART + e * 1024);
            u32x2 b;
            b[0] = cvt_pk_bf16(t4[0], t4[1]);
            b[1] = cvt_pk_bf16(t4[2], t4[3]);
            o[e] = __builtin_amdgcn_mfma_f32_16x16x16bf16_1k(__builtin_bit_cast(s16x4, pa), __builtin_bit_cast(s16x4, b), f32x4{0.f, 0.f, 0.f, 0.f}, 0, 0, 0);
        }
        unsigned short* op = out + ((size_t)(wg * HPW + (wave >> 1)) * SQ + 16 * (wave & 1) + 4 * g) * DH + 8 * c;
#pragma unroll
        for (int i = 0; i < 4; ++i) {
            u32x4 w4;
#pragma unroll
            for (int m = 0; m < 4; ++m) w4[m] = cvt_pk_bf16(o[2 * m][i], o[2 * m + 1][i]);
            *(u32x4*)(op + i * DH) = w4;
        }
    }
}

extern "C" void kernel_launch(void* const* d_in, const int* in_sizes, int n_in,
                              void* d_out, int out_size, void* d_ws, size_t ws_size,
                              hipStream_t stream) {
    const unsigned short* q = (const unsigned short*)d_in[0];
    const unsigned short* k = (const unsigned short*)d_in[1];
    const unsigned short* v = (const unsigned short*)d_in[2];
    const unsigned short* e = (const unsigned short*)d_in[3];
    const unsigned short* f = (const unsigned short*)d_in[4];
    auto kern = linformer_kernel<DEPTH, AUX>;
    (void)hipFuncSetAttribute((const void*)kern, hipFuncAttributeMaxDynamicSharedMemorySize, LDS_BYTES);
    kern<<<NWG, NW * 64, LDS_BYTES, stream>>>(q, k, v, e, f, (unsigned short*)d_out
        );
}
```

```cpp
#include <hip/hip_runtime.h>
#include <hip/hip_bf16.h>
#include <stdint.h>

typedef unsigned u32x4 __attribute__((ext_vector_type(4)));
typedef unsigned u32x2 __attribute__((ext_vector_type(2)));
typedef float f32x4 __attribute__((ext_vector_type(4)));
typedef short s16x4 __attribute__((ext_vector_type(4)));
typedef __bf16 bf16x8_t __attribute__((ext_vector_type(8)));

#define DEPTH 4
#define AUX 2
#define STAMP 0
#define STAUX 16
#define TAU 1760

constexpr int NBH = 512, SQ = 32, SKV = 4096, DH = 128, RK = 16;
constexpr int NW = 4;
constexpr int NWG = 256;
constexpr int HPW = NBH / NWG;
constexpr int CPS = (SKV / 32) / NW;
constexpr int NC = 2 * HPW * CPS;
constexpr int KROW = 528;
constexpr int KPART = 16 * KROW;
constexpr int VPART = 8 * 64 * 16;
constexpr int HEADBUF = NW * (KPART + VPART);
constexpr int LDS_BYTES = HPW * HEADBUF;

__device__ __forceinline__ unsigned cvt_pk_bf16(float lo, float hi) {
    typedef __bf16 bf2 __attribute__((ext_vector_type(2)));
    bf2 r; r[0] = (__bf16)lo; r[1] = (__bf16)hi;
    return __builtin_bit_cast(unsigned, r);
}
__device__ __forceinline__ float bf16_round(float x) { return (float)((__bf16)x); }

template <int kDEPTH, int kAUX>
__global__ void __launch_bounds__(NW * 64, 1)
linformer_kernel(const unsigned short* __restrict__ q, const unsigned short* __restrict__ k,
                 const unsigned short* __restrict__ v, const unsigned short* __restrict__ ep,
                 const unsigned short* __restrict__ fp, unsigned short* __restrict__ out
                 ) {
    static_assert(CPS % kDEPTH == 0, "");
    static_assert(HPW * 2 == NW, "epilogue: one (head, q tile) per wave");
    extern __shared__ __attribute__((aligned(16))) unsigned char lds[];

    const int wg = blockIdx.x;
    const int tid = threadIdx.x, lane = tid & 63;
    const int wave = __builtin_amdgcn_readfirstlane(tid >> 6);
    const int g = lane >> 4, c = lane & 15;
#define LATE(now_) do {} while (0)

    const int voffX = g * 2048 + c * 16;
    const int voffP = c * (SKV * 2) + g * 16;

    const unsigned t0lo = (unsigned)__builtin_amdgcn_s_memrealtime();
    unsigned nxt = (((unsigned)(wg * NW + wave) * 2654435761u) >> 16) % (unsigned)(TAU ? TAU : 1);
#define PACE() do { if (TAU) { unsigned now_ = ((unsigned)__builtin_amdgcn_s_memrealtime() - t0lo) << 4; \
        while ((int)(now_ - nxt) < 0) { __builtin_amdgcn_s_sleep(1); now_ = ((unsigned)__builtin_amdgcn_s_memrealtime() - t0lo) << 4; } \
        LATE(now_); nxt += TAU; } } while (0)

    u32x4 xb[kDEPTH][8], ab[kDEPTH];
    f32x4 acc[8];

#define ISSUE(s_, cn_) do { const int n_ = (cn_); const int seg_ = n_ / CPS, cc_ = (n_ % CPS) * NW + wave; \
        const bool isv_ = seg_ & 1; \
        const __amdgpu_buffer_rsrc_t rX_ = __builtin_amdgcn_make_buffer_rsrc((void*)(isv_ ? v : k), 0, NBH * SKV * DH * 2, 0x00020000); \
        const __amdgpu_buffer_rsrc_t rP_ = __builtin_amdgcn_make_buffer_rsrc((void*)(isv_ ? fp : ep), 0, RK * SKV * 2, 0x00020000); \
        const int sx_ = (wg * HPW + (seg_ >> 1)) * (SKV * DH * 2) + cc_ * 8192; \
        ab[s_] = __builtin_amdgcn_raw_buffer_load_b128(rP_, voffP, cc_ * 64, 0); \
        _Pragma("unroll") for (int i = 0; i < 8; ++i) xb[s_][i] = __builtin_amdgcn_raw_buffer_load_b128(rX_, voffX + i * 256, sx_, kAUX); } while (0)
#define CONSUME(s_) do { \
        _Pragma("unroll") for (int w = 0; w < 4; ++w) { u32x4 lo, hi; \
            _Pragma("unroll") for (int m = 0; m < 4; ++m) { \
                lo[m] = __builtin_amdgcn_perm(xb[s_][2 * m + 1][w], xb[s_][2 * m][w], 0x05040100u); \
                hi[m] = __builtin_amdgcn_perm(xb[s_][2 * m + 1][w], xb[s_][2 * m][w], 0x07060302u); } \
            acc[2 * w]     = __builtin_amdgcn_mfma_f32_16x16x32_bf16(__builtin_bit_cast(bf16x8_t, ab[s_]), __builtin_bit_cast(bf16x8_t, lo), acc[2 * w], 0, 0, 0); \
            acc[2 * w + 1] = __builtin_amdgcn_mfma_f32_16x16x32_bf16(__builtin_bit_cast(bf16x8_t, ab[s_]), __builtin_bit_cast(bf16x8_t, hi), acc[2 * w + 1], 0, 0, 0); \
        } } while (0)
#define FLUSH_K(head_) do { unsigned char* p = lds + (head_) * HEADBUF + wave * KPART + (4 * g) * KROW + c * 16; \
        _Pragma("unroll") for (int i = 0; i < 4; ++i) { \
            *(f32x4*)(p + i * KROW)       = f32x4{acc[0][i], acc[1][i], acc[2][i], acc[3][i]}; \
            *(f32x4*)(p + i * KROW + 256) = f32x4{acc[4][i], acc[5][i], acc[6][i], acc[7][i]}; } } while (0)
#define FLUSH_V(head_) do { unsigned char* p = lds + (head_) * HEADBUF + NW * KPART + wave * VPART + lane * 16; \
        _Pragma("unroll") for (int e = 0; e < 8; ++e) *(f32x4*)(p + e * 1024) = acc[e]; } while (0)

#pragma unroll
    for (int s = 0; s < kDEPTH; ++s) { PACE(); ISSUE(s, s); __builtin_amdgcn_sched_barrier(0); }
#pragma unroll
    for (int e = 0; e < 8; ++e) acc[e] = f32x4{0.f, 0.f, 0.f, 0.f};

    for (int n = 0; n < NC - kDEPTH; n += kDEPTH) {
#pragma unroll
        for (int s = 0; s < kDEPTH; ++s) {
            CONSUME(s); __builtin_amdgcn_sched_barrier(0);
            PACE();
            ISSUE(s, n + s + kDEPTH); __builtin_amdgcn_sched_barrier(0);
        }
        if (((n + kDEPTH) % CPS) == 0) {
            const int seg = n / CPS;
            if (seg & 1) FLUSH_V(seg >> 1); else FLUSH_K(seg >> 1);
#pragma unroll
            for (int e = 0; e < 8; ++e) acc[e] = f32x4{0.f, 0.f, 0.f, 0.f};
        }
    }
    u32x4 qf[4];
    {
        const unsigned short* qp = q + ((size_t)(wg * HPW + (wave >> 1)) * SQ + 16 * (wave & 1) + c) * DH + 32 * g;
#pragma unroll
        for (int s = 0; s < 4; ++s) qf[s] = *(const u32x4*)(qp + 8 * s);
    }
    __builtin_amdgcn_sched_barrier(0);
#pragma unroll
    for (int s = 0; s < kDEPTH; ++s) { CONSUME(s); }
    FLUSH_V(HPW - 1);
    asm volatile("s_waitcnt lgkmcnt(0)" ::: "memory");
    __builtin_amdgcn_s_barrier();
    asm volatile("" ::: "memory");
    {
        const unsigned char* hb = lds + (wave >> 1) * HEADBUF;
        f32x4 st = f32x4{0.f, 0.f, 0.f, 0.f};
#pragma unroll
        for (int s = 0; s < 4; ++s) {
            const unsigned char* p0 = hb + c * KROW + (4 * g + s) * 16;
            f32x4 a0 = *(const f32x4*)(p0), a1 = *(const f32x4*)(p0 + 256);
#pragma unroll
            for (int w = 1; w < NW; ++w) { a0 += *(const f32x4*)(p0 + w * KPART); a1 += *(const f32x4*)(p0 + w * KPART + 256); }
            u32x4 a;
            a[0] = cvt_pk_bf16(a0[0], a0[1]); a[1] = cvt_pk_bf16(a0[2], a0[3]);
            a[2] = cvt_pk_bf16(a1[0], a1[1]); a[3] = cvt_pk_bf16(a1[2], a1[3]);
            st = __builtin_amdgcn_mfma_f32_16x16x32_bf16(__builtin_bit_cast(bf16x8_t, a), __builtin_bit_cast(bf16x8_t, qf[s]), st, 0, 0, 0);
        }
        float t[4];
#pragma unroll
        for (int i = 0; i < 4; ++i) t[i] = bf16_round(bf16_round(st[i]) * 0.08838834764831845f);
        float mx = fmaxf(fmaxf(t[0], t[1]), fmaxf(t[2], t[3]));
        mx = fmaxf(mx, __shfl_xor(mx, 16));
        mx = fmaxf(mx, __shfl_xor(mx, 32));
        float ex[4], sum = 0.f;
#pragma unroll
        for (int i = 0; i < 4; ++i) { ex[i] = __expf(t[i] - mx); sum += ex[i]; }
        sum += __shfl_xor(sum, 16);
        sum += __shfl_xor(sum, 32);
        const float inv = 1.0f / sum;
        u32x2 pa;
        pa[0] = cvt_pk_bf16(ex[0] * inv, ex[1] * inv);
        pa[1] = cvt_pk_bf16(ex[2] * inv, ex[3] * inv);
        f32x4 o[8];
        const unsigned char* pv = hb + NW * KPART + lane * 16;
#pragma unroll
        for (int e = 0; e < 8; ++e) {
            f32x4 t4 = *(const f32x4*)(pv + e * 1024);
#pragma unroll
            for (int w = 1; w < NW; ++w) t4 += *(const f32x4*)(pv + w * VPART + e * 1024);
            u32x2 b;
            b[0] = cvt_pk_bf16(t4[0], t4[1]);
            b[1] = cvt_pk_bf16(t4[2], t4[3]);
            o[e] = __builtin_amdgcn_mfma_f32_16x16x16bf16_1k(__builtin_bit_cast(s16x4, pa), __builtin_bit_cast(s16x4, b), f32x4{0.f, 0.f, 0.f, 0.f}, 0, 0, 0);
        }
        const __amdgpu_buffer_rsrc_t rO = __builtin_amdgcn_make_buffer_rsrc((void*)out, 0, NBH * SQ * DH * 2, 0x00020000);
        unsigned short* op = out + ((size_t)(wg * HPW + (wave >> 1)) * SQ + 16 * (wave & 1) + 4 * g) * DH + 8 * c;
#pragma unroll
        for (int i = 0; i < 4; ++i) {
            u32x4 w4;
#pragma unroll
            for (int m = 0; m < 4; ++m) w4[m] = cvt_pk_bf16(o[2 * m][i], o[2 * m + 1][i]);
            if (STAUX < 0) *(u32x4*)(op + i * DH) = w4;
            else __builtin_amdgcn_raw_buffer_store_b128(w4, rO, (int)((char*)(op + i * DH) - (char*)out), 0, STAUX);
        }
    }
}

extern "C" void kernel_launch(void* const* d_in, const int* in_sizes, int n_in,
                              void* d_out, int out_size, void* d_ws, size_t ws_size,
                              hipStream_t stream) {
    const unsigned short* q = (const unsigned short*)d_in[0];
    const unsigned short* k = (const unsigned short*)d_in[1];
    const unsigned short* v = (const unsigned short*)d_in[2];
    const unsigned short* e = (const unsigned short*)d_in[3];
    const unsigned short* f = (const unsigned short*)d_in[4];
    auto kern = linformer_kernel<DEPTH, AUX>;
    (void)hipFuncSetAttribute((const void*)kern, hipFuncAttributeMaxDynamicSharedMemorySize, LDS_BYTES);
    kern<<<NWG, NW * 64, LDS_BYTES, stream>>>(q, k, v, e, f, (unsigned short*)d_out
        );
}
```

```cpp
#include <hip/hip_runtime.h>
#include <hip/hip_bf16.h>
#include <stdint.h>

typedef unsigned u32x4 __attribute__((ext_vector_type(4)));
typedef unsigned u32x2 __attribute__((ext_vector_type(2)));
typedef float f32x4 __attribute__((ext_vector_type(4)));
typedef short s16x4 __attribute__((ext_vector_type(4)));
typedef __bf16 bf16x8_t __attribute__((ext_vector_type(8)));
typedef __attribute__((address_space(3))) unsigned lds_u32;

constexpr int NBH = 512, SQ = 32, SKV = 4096, DH = 128, RK = 16;
constexpr int NW = 4;
constexpr int NH = 4;
constexpr int NWG = 256;
constexpr int HPW = NBH / NWG;
constexpr int CPS = (SKV / 32) / NW;
constexpr int NC = 2 * HPW * CPS;
constexpr int DEPTH = 4;
constexpr int TAU = 1756;
constexpr int FORGIVE = 12;
constexpr int QLEAD = 390;
constexpr int KROW = 528;
constexpr int KPART = 16 * KROW;
constexpr int VPART = 8 * 64 * 16;
constexpr int HEADBUF = NW * (KPART + VPART);
constexpr int CNT_OFF = HPW * HEADBUF;
constexpr int LDS_BYTES = CNT_OFF + 64;
static_assert(CPS % DEPTH == 0 && HPW == 2 && NH == 2 * HPW, "work split");

__device__ __forceinline__ unsigned cvt_pk_bf16(float lo, float hi) {
    typedef __bf16 bf2 __attribute__((ext_vector_type(2)));
    bf2 r; r[0] = (__bf16)lo; r[1] = (__bf16)hi;
    return __builtin_bit_cast(unsigned, r);
}
__device__ __forceinline__ float bf16_round(float x) { return (float)((__bf16)x); }
__device__ __forceinline__ float xrow_max(float x) {
    auto s = __builtin_amdgcn_permlane16_swap(__float_as_uint(x), __float_as_uint(x), false, false);
    x = fmaxf(__uint_as_float(s[0]), __uint_as_float(s[1]));
    auto t = __builtin_amdgcn_permlane32_swap(__float_as_uint(x), __float_as_uint(x), false, false);
    return fmaxf(__uint_as_float(t[0]), __uint_as_float(t[1]));
}
__device__ __forceinline__ float xrow_sum(float x) {
    auto s = __builtin_amdgcn_permlane16_swap(__float_as_uint(x), __float_as_uint(x), false, false);
    x = __uint_as_float(s[0]) + __uint_as_float(s[1]);
    auto t = __builtin_amdgcn_permlane32_swap(__float_as_uint(x), __float_as_uint(x), false, false);
    return __uint_as_float(t[0]) + __uint_as_float(t[1]);
}

__global__ void __launch_bounds__((NW + NH) * 64, 2)
linformer_kernel(const unsigned short* __restrict__ q, const unsigned short* __restrict__ k,
                 const unsigned short* __restrict__ v, const unsigned short* __restrict__ ep,
                 const unsigned short* __restrict__ fp, unsigned short* __restrict__ out) {
    extern __shared__ __attribute__((aligned(16))) unsigned char lds[];
    volatile lds_u32* cnt = (volatile lds_u32*)(lds + CNT_OFF);

    const unsigned t0 = (unsigned)__builtin_amdgcn_s_memrealtime();
#define NOW() ((unsigned)__builtin_amdgcn_s_memrealtime() - t0)
    const int wg = blockIdx.x;
    const int tid = threadIdx.x, lane = tid & 63;
    const int wave = __builtin_amdgcn_readfirstlane(tid >> 6);
    const int g = lane >> 4, c = lane & 15;

    if (wave >= NW) {
        const int hd = (wave - NW) >> 1, t = (wave - NW) & 1;
        if (wave == NW && lane < 4) cnt[lane] = 0u;
        {
            const __amdgpu_buffer_rsrc_t rP = __builtin_amdgcn_make_buffer_rsrc((void*)(hd ? fp : ep), 0, RK * SKV * 2, 0x00020000);
            const int off = ((wg >> 3) & 31) * 4096 + t * 2048 + lane * 16;
            const u32x4 p0 = __builtin_amdgcn_raw_buffer_load_b128(rP, off, 0, 0), p1 = __builtin_amdgcn_raw_buffer_load_b128(rP, off + 1024, 0, 0);
            __builtin_amdgcn_sched_barrier(0);
            asm volatile("s_waitcnt lgkmcnt(0)" ::: "memory");
            __builtin_amdgcn_s_barrier();
            asm volatile("" :: "v"(p0), "v"(p1) : "memory");
        }
        while (NOW() < (unsigned)(NC * TAU / 16 - QLEAD)) __builtin_amdgcn_s_sleep(32);
        u32x4 qf[4];
        {
            const __amdgpu_buffer_rsrc_t rQ = __builtin_amdgcn_make_buffer_rsrc((void*)q, 0, NBH * SQ * DH * 2, 0x00020000);
            const int qo = (((wg * HPW + hd) * SQ + 16 * t + c) * DH + 32 * g) * 2;
#pragma unroll
            for (int s = 0; s < 4; ++s) qf[s] = __builtin_amdgcn_raw_buffer_load_b128(rQ, qo + 16 * s, 0, 2);
        }
        const unsigned char* hb = lds + hd * HEADBUF;
        while (__builtin_amdgcn_readfirstlane(cnt[hd]) != NW) __builtin_amdgcn_s_sleep(1);
        asm volatile("" ::: "memory");
        f32x4 st = f32x4{0.f, 0.f, 0.f, 0.f};
#pragma unroll
        for (int s = 0; s < 4; ++s) {
            const unsigned char* p0 = hb + c * KROW + (4 * g + s) * 16;
            f32x4 a0 = *(const f32x4*)(p0), a1 = *(const f32x4*)(p0 + 256);
#pragma unroll
            for (int w = 1; w < NW; ++w) { a0 += *(const f32x4*)(p0 + w * KPART); a1 += *(const f32x4*)(p0 + w * KPART + 256); }
            u32x4 a;
            a[0] = cvt_pk_bf16(a0[0], a0[1]); a[1] = cvt_pk_bf16(a0[2], a0[3]);
            a[2] = cvt_pk_bf16(a1[0], a1[1]); a[3] = cvt_pk_bf16(a1[2], a1[3]);
            st = __builtin_amdgcn_mfma_f32_16x16x32_bf16(__builtin_bit_cast(bf16x8_t, a), __builtin_bit_cast(bf16x8_t, qf[s]), st, 0, 0, 0);
        }
        float tt[4];
#pragma unroll
        for (int i = 0; i < 4; ++i) tt[i] = bf16_round(bf16_round(st[i]) * 0.08838834764831845f);
        const float mx = xrow_max(fmaxf(fmaxf(tt[0], tt[1]), fmaxf(tt[2], tt[3])));
        float ex[4], sum = 0.f;
#pragma unroll
        for (int i = 0; i < 4; ++i) { ex[i] = __expf(tt[i] - mx); sum += ex[i]; }
        const float inv = 1.0f / xrow_sum(sum);
        u32x2 pa;
        pa[0] = cvt_pk_bf16(ex[0] * inv, ex[1] * inv);
        pa[1] = cvt_pk_bf16(ex[2] * inv, ex[3] * inv);
        while (__builtin_amdgcn_readfirstlane(cnt[2 + hd]) != NW) __builtin_amdgcn_s_sleep(1);
        asm volatile("" ::: "memory");
        f32x4 o[8];
        const unsigned char* pv = hb + NW * KPART + lane * 16;
#pragma unroll
        for (int e = 0; e < 8; ++e) {
            f32x4 t4 = *(const f32x4*)(pv + e * 1024);
#pragma unroll
            for (int w = 1; w < NW; ++w) t4 += *(const f32x4*)(pv + w * VPART + e * 1024);
            u32x2 b;
            b[0] = cvt_pk_bf16(t4[0], t4[1]);
            b[1] = cvt_pk_bf16(t4[2], t4[3]);
            o[e] = __builtin_amdgcn_mfma_f32_16x16x16bf16_1k(__builtin_bit_cast(s16x4, pa), __builtin_bit_cast(s16x4, b), f32x4{0.f, 0.f, 0.f, 0.f}, 0, 0, 0);
        }
        unsigned short* op = out + ((size_t)(wg * HPW + hd) * SQ + 16 * t + 4 * g) * DH + 8 * c;
#pragma unroll
        for (int i = 0; i < 4; ++i) {
            u32x4 w4;
#pragma unroll
            for (int m = 0; m < 4; ++m) w4[m] = cvt_pk_bf16(o[2 * m][i], o[2 * m + 1][i]);
            *(u32x4*)(op + i * DH) = w4;
        }
        return;
    }

    const int voffX = g * 2048 + c * 16;
    const int voffP = c * (SKV * 2) + g * 16;

    unsigned nxt = (unsigned)(wg * NW + wave) * (TAU / 2) / (unsigned)(NWG * NW);
    int npace = 0;
#define PACE() do { unsigned now_ = NOW() << 4; \
        if (npace < FORGIVE) { if ((int)(now_ - nxt) > 0) nxt = now_; ++npace; } \
        while ((int)(now_ - nxt) < 0) { __builtin_amdgcn_s_sleep(1); now_ = NOW() << 4; } \
        nxt += TAU / 2; } while (0)

    u32x4 xb[DEPTH][8], ab[DEPTH];
    f32x4 acc[8];

#define ISSUE(s_, cn_) do { const int n_ = (cn_); const int seg_ = n_ / CPS, cc_ = (n_ % CPS) * NW + wave; \
        const bool isv_ = seg_ & 1; \
        const __amdgpu_buffer_rsrc_t rX_ = __builtin_amdgcn_make_buffer_rsrc((void*)(isv_ ? v : k), 0, NBH * SKV * DH * 2, 0x00020000); \
        const __amdgpu_buffer_rsrc_t rP_ = __builtin_amdgcn_make_buffer_rsrc((void*)(isv_ ? fp : ep), 0, RK * SKV * 2, 0x00020000); \
        const int sx_ = (wg * HPW + (seg_ >> 1)) * (SKV * DH * 2) + cc_ * 8192; \
        PACE(); \
        ab[s_] = __builtin_amdgcn_raw_buffer_load_b128(rP_, voffP, cc_ * 64, 0); \
        _Pragma("unroll") for (int i = 0; i < 8; ++i) { if (i == 4) { __builtin_amdgcn_sched_barrier(0); PACE(); } \
            xb[s_][i] = __builtin_amdgcn_raw_buffer_load_b128(rX_, voffX + i * 256, sx_, 2  ); } } while (0)
#define CONSUME(s_) do { \
        _Pragma("unroll") for (int w = 0; w < 4; ++w) { u32x4 lo, hi; \
            _Pragma("unroll") for (int m = 0; m < 4; ++m) { \
                lo[m] = __builtin_amdgcn_perm(xb[s_][2 * m + 1][w], xb[s_][2 * m][w], 0x05040100u); \
                hi[m] = __builtin_amdgcn_perm(xb[s_][2 * m + 1][w], xb[s_][2 * m][w], 0x07060302u); } \
            acc[2 * w]     = __builtin_amdgcn_mfma_f32_16x16x32_bf16(__builtin_bit_cast(bf16x8_t, ab[s_]), __builtin_bit_cast(bf16x8_t, lo), acc[2 * w], 0, 0, 0); \
            acc[2 * w + 1] = __builtin_amdgcn_mfma_f32_16x16x32_bf16(__builtin_bit_cast(bf16x8_t, ab[s_]), __builtin_bit_cast(bf16x8_t, hi), acc[2 * w + 1], 0, 0, 0); \
        } } while (0)
#define FLUSH(seg_) do { const int head_ = (seg_) >> 1; \
        if ((seg_) & 1) { unsigned char* p = lds + head_ * HEADBUF + NW * KPART + wave * VPART + lane * 16; \
            _Pragma("unroll") for (int e = 0; e < 8; ++e) *(f32x4*)(p + e * 1024) = acc[e]; \
        } else { unsigned char* p = lds + head_ * HEADBUF + wave * KPART + (4 * g) * KROW + c * 16; \
            _Pragma("unroll") for (int i = 0; i < 4; ++i) { \
                *(f32x4*)(p + i * KROW)       = f32x4{acc[0][i], acc[1][i], acc[2][i], acc[3][i]}; \
                *(f32x4*)(p + i * KROW + 256) = f32x4{acc[4][i], acc[5][i], acc[6][i], acc[7][i]}; } } \
        asm volatile("s_waitcnt lgkmcnt(0)" ::: "memory"); \
        if (lane == 0) __hip_atomic_fetch_add((lds_u32*)(cnt + ((seg_) & 1) * 2 + head_), 1u, __ATOMIC_RELAXED, __HIP_MEMORY_SCOPE_WORKGROUP); } while (0)

#pragma unroll
    for (int s = 0; s < DEPTH; ++s) {
        ISSUE(s, s); __builtin_amdgcn_sched_barrier(0);
        if (s == 0) __builtin_amdgcn_s_barrier();
    }
#pragma unroll
    for (int e = 0; e < 8; ++e) acc[e] = f32x4{0.f, 0.f, 0.f, 0.f};

    for (int n = 0; n < NC - DEPTH; n += DEPTH) {
#pragma unroll
        for (int s = 0; s < DEPTH; ++s) {
            CONSUME(s); __builtin_amdgcn_sched_barrier(0);
            ISSUE(s, n + s + DEPTH); __builtin_amdgcn_sched_barrier(0);
        }
        if (((n + DEPTH) % CPS) == 0) {
            FLUSH(n / CPS);
#pragma unroll
            for (int e = 0; e < 8; ++e) acc[e] = f32x4{0.f, 0.f, 0.f, 0.f};
        }
    }
#pragma unroll
    for (int s = 0; s < DEPTH; ++s) { CONSUME(s); }
    FLUSH(2 * HPW - 1);
}

extern "C" void kernel_launch(void* const* d_in, const int* in_sizes, int n_in,
                              void* d_out, int out_size, void* d_ws, size_t ws_size,
                              hipStream_t stream) {
    const unsigned short* q = (const unsigned short*)d_in[0];
    const unsigned short* k = (const unsigned short*)d_in[1];
    const unsigned short* v = (const unsigned short*)d_in[2];
    const unsigned short* e = (const unsigned short*)d_in[3];
    const unsigned short* f = (const unsigned short*)d_in[4];
    (void)hipFuncSetAttribute((const void*)linformer_kernel, hipFuncAttributeMaxDynamicSharedMemorySize, LDS_BYTES);
    linformer_kernel<<<NWG, (NW + NH) * 64, LDS_BYTES, stream>>>(q, k, v, e, f, (unsigned short*)d_out);
}
```
